```python
import torch

def _softmax(x, axis=-1):
    return torch.softmax(x, dim=axis)
def _gelu(x):
    return torch.nn.functional.gelu(x, approximate="tanh")
def _silu(x):
    return torch.nn.functional.silu(x)
def _sigmoid(x):
    return torch.sigmoid(x)
def _layernorm(x, axis=-1, eps=1e-5):
    m = x.mean(dim=axis, keepdim=True)
    v = x.var(dim=axis, keepdim=True, unbiased=False)
    return (x - m) / torch.sqrt(v + eps)
def _scatter_add(tgt, idx, src, axis=0):
    return tgt.clone().index_add_(axis, idx, src)
def _segment_sum(src, seg, n):
    out = torch.zeros((n,) + src.shape[1:], dtype=src.dtype)
    return out.index_add_(0, seg, src)
def _pad(x, pw):
    flat = tuple(v for pair in reversed(pw) for v in pair)
    return torch.nn.functional.pad(x, flat)

kernel_name = 'concat_heads__gqa_block_cached__gqa_block_cached__near_ridge'

def setup_inputs():

    _w = (lambda a: (lambda t: t.to(torch.bfloat16) if t.is_floating_point() else t)(torch.as_tensor(a)))
    def _A_setup():
        B, Sq, D = 8, 96, 448
        Hq, Hkv, Sk, Dh = 16, 8, 1024, 256
        assert Hq % Hkv == 0, (Hq, Hkv)
        return {
            "x": _w((lambda *s: torch.randn(*s, dtype=torch.bfloat16) * 0.8)(B, Sq, D)),
            "k_cache": _w((lambda *s: torch.randn(*s, dtype=torch.bfloat16) * 0.8)(B, Hkv, Sk, Dh)),
            "v_cache": _w((lambda *s: torch.randn(*s, dtype=torch.bfloat16) * 0.05)(B, Hkv, Sk, Dh)),
            "wq": _w((lambda *s: torch.randn(*s, dtype=torch.bfloat16) * 0.8)(D, Hq * Dh) * (D ** -0.5)),
            "wo": _w((lambda *s: torch.randn(*s, dtype=torch.bfloat16) * 0.05)(Hq * Dh, D) * ((Hq * Dh) ** -0.5)),
        }
    _A_in = _A_setup()

    def _B_setup():
        B, Sq, D = 8, 96, 448
        Hq, Hkv, Sk, Dh = 16, 8, 1024, 256
        assert Hq % Hkv == 0, (Hq, Hkv)
        return {
            "x": _w((lambda *s: torch.randn(*s, dtype=torch.bfloat16) * 0.8)(B, Sq, D)),
            "k_cache": _w((lambda *s: torch.randn(*s, dtype=torch.bfloat16) * 0.8)(B, Hkv, Sk, Dh)),
            "v_cache": _w((lambda *s: torch.randn(*s, dtype=torch.bfloat16) * 0.05)(B, Hkv, Sk, Dh)),
            "wq": _w((lambda *s: torch.randn(*s, dtype=torch.bfloat16) * 0.8)(D, Hq * Dh) * (D ** -0.5)),
            "wo": _w((lambda *s: torch.randn(*s, dtype=torch.bfloat16) * 0.05)(Hq * Dh, D) * ((Hq * Dh) ** -0.5)),
        }
    _B_in = _B_setup()

    return {
        "A_x": _A_in["x"],
        "A_k_cache": _A_in["k_cache"],
        "A_v_cache": _A_in["v_cache"],
        "A_wq": _A_in["wq"],
        "A_wo": _A_in["wo"],
        "B_x": _B_in["x"],
        "B_k_cache": _B_in["k_cache"],
        "B_v_cache": _B_in["v_cache"],
        "B_wq": _B_in["wq"],
        "B_wo": _B_in["wo"],
    }

def reference(A_x, A_k_cache, A_v_cache, A_wq, A_wo, B_x, B_k_cache, B_v_cache, B_wq, B_wo):
    def _A_ref(x, k_cache, v_cache, wq, wo):
        B, Sq, D = x.shape
        Hq, Dh = 16, 256
        Hkv = k_cache.shape[1]
        rep = Hq // Hkv
        q = (lambda x, *d: x.permute(*d))((x @ wq).reshape(B, Sq, Hq, Dh), 0, 2, 1, 3)
        k = (lambda x, n, axis: torch.repeat_interleave(x, n, dim=axis))(k_cache, rep, axis=1)
        v = (lambda x, n, axis: torch.repeat_interleave(x, n, dim=axis))(v_cache, rep, axis=1)
        scores = torch.einsum("bhqd,bhkd->bhqk", q, k) * (Dh ** -0.5)
        p = _softmax(scores, axis=-1)
        o = torch.einsum("bhqk,bhkd->bhqd", p, v)
        return (lambda x, *d: x.permute(*d))(o, 0, 2, 1, 3).reshape(B, Sq, Hq * Dh) @ wo

    def _B_ref(x, k_cache, v_cache, wq, wo):
        B, Sq, D = x.shape
        Hq, Dh = 16, 256
        Hkv = k_cache.shape[1]
        rep = Hq // Hkv
        q = (lambda x, *d: x.permute(*d))((x @ wq).reshape(B, Sq, Hq, Dh), 0, 2, 1, 3)
        k = (lambda x, n, axis: torch.repeat_interleave(x, n, dim=axis))(k_cache, rep, axis=1)
        v = (lambda x, n, axis: torch.repeat_interleave(x, n, dim=axis))(v_cache, rep, axis=1)
        scores = torch.einsum("bhqd,bhkd->bhqk", q, k) * (Dh ** -0.5)
        p = _softmax(scores, axis=-1)
        o = torch.einsum("bhqk,bhkd->bhqd", p, v)
        return (lambda x, *d: x.permute(*d))(o, 0, 2, 1, 3).reshape(B, Sq, Hq * Dh) @ wo

    a = _A_ref(A_x, A_k_cache, A_v_cache, A_wq, A_wo)
    b = _B_ref(B_x, B_k_cache, B_v_cache, B_wq, B_wo)
    return torch.concatenate([a, b], axis=-1)
```

```cpp
#include <hip/hip_runtime.h>
#include <hip/hip_bf16.h>

typedef __hip_bfloat16 bf16;
using bf16x8 = __attribute__((ext_vector_type(8))) short;
using s16x4  = __attribute__((ext_vector_type(4))) short;
using f32x4  = __attribute__((ext_vector_type(4))) float;
using u32x2  = __attribute__((ext_vector_type(2))) unsigned;
typedef __attribute__((address_space(3))) s16x4* lds_s16x4_p;

constexpr int NB = 8, SQ = 96, DM = 448, HQ = 16, HKV = 8, SK = 1024, DH = 256;
constexpr int MT = 128;
constexpr int NT = SK / MT;
constexpr int KBUF = 0, VBUF = 65536, SCR = 131072, LDS_BYTES = 163840;
constexpr float SCL = 0.0625f * 1.4426950408889634f;

struct Ptrs { const bf16* x[2]; const bf16* k[2]; const bf16* v[2]; const bf16* wq[2]; };
struct Ptrs2 { const bf16* wo[2]; };

__device__ __forceinline__ void dma16(const void* src, void* lds) {
  __builtin_amdgcn_global_load_lds((const unsigned*)src, (unsigned*)lds, 16, 0, 0);
}
typedef __attribute__((address_space(3))) void* lds_void_p;
__device__ __forceinline__ __amdgpu_buffer_rsrc_t mk_rsrc(const void* p, unsigned bytes) {
  return __builtin_amdgcn_make_buffer_rsrc(const_cast<void*>(p), 0, bytes, 0x00020000);
}
__device__ __forceinline__ void bdma16(__amdgpu_buffer_rsrc_t r, int voff, int soff, void* lds) {
  __builtin_amdgcn_raw_ptr_buffer_load_lds(r, (lds_void_p)lds, 16, voff, soff, 0, 0);
}
__device__ __forceinline__ s16x4 tr_read(const char* p) {
  return __builtin_amdgcn_ds_read_tr16_b64_v4i16((lds_s16x4_p)p);
}
__device__ __forceinline__ unsigned pk2(float lo, float hi) {
  union { __hip_bfloat162 h; unsigned u; } c;
  c.h = __float22bfloat162_rn(float2{lo, hi});
  return c.u;
}
__device__ __forceinline__ f32x4 mfma16(bf16x8 a, bf16x8 b, f32x4 c) {
  return __builtin_amdgcn_mfma_f32_16x16x32_bf16(a, b, c, 0, 0, 0);
}
__device__ __forceinline__ bf16x8 cat(s16x4 l, s16x4 h) {
  return (bf16x8){l[0], l[1], l[2], l[3], h[0], h[1], h[2], h[3]};
}

__global__ __launch_bounds__(512, 2) void attn_kernel(Ptrs P, bf16* __restrict__ Ows) {
  extern __shared__ __attribute__((aligned(16))) char sm[];
  const int tid = threadIdx.x, w = tid >> 6, l = tid & 63, g = l >> 4, i = l & 15;
  const int bid = blockIdx.x;
  const int grp = ((bid >> 4) << 3) | (bid & 7), pr = (bid >> 3) & 1;
  const int inst = grp >> 6, b = (grp >> 3) & 7, kvh = grp & 7, h = kvh * 2 + pr;
  const bf16* xg = P.x[inst] + (size_t)b * SQ * DM;
  const bf16* wg = P.wq[inst] + h * DH;
  const bf16* kg = P.k[inst] + (size_t)(b * HKV + kvh) * SK * DH;
  const bf16* vg = P.v[inst] + (size_t)(b * HKV + kvh) * SK * DH;

  const int wu = __builtin_amdgcn_readfirstlane(w);
  const int fi = (0x1320 >> ((i >> 2) * 4)) & 3;

  {
    f32x4 acc[2][6];
#pragma unroll
    for (int d = 0; d < 2; ++d)
#pragma unroll
      for (int r = 0; r < 6; ++r) acc[d][r] = f32x4{0.f, 0.f, 0.f, 0.f};
    const __amdgpu_buffer_rsrc_t wrs = mk_rsrc(wg, (DM * HQ * DH - h * DH) * 2);
    int wvo;
    {
      const int kp = (tid >> 8) * 8 + ((tid >> 1) & 7);
      const int kr = (kp & 3) | ((kp & 4) << 1) | ((kp & 8) >> 1);
      wvo = kr * (HQ * DH * 2) + (((tid >> 4) & 15) * 2 + (tid & 1)) * 16;
    }
    auto stageW = [&](int kc, int buf) {
#pragma unroll
      for (int it = 0; it < 2; ++it)
        bdma16(wrs, wvo, (kc * 32 + it * 16) * (HQ * DH * 2), sm + VBUF + buf * 16384 + it * 8192 + wu * 1024);
    };
    stageW(0, 0);
    const int wrd = (g >> 1) * 8192 + ((g & 1) * 4 + (i >> 2)) * 32 + (l & 3) * 8 + wu * 512;
    for (int kc = 0; kc < DM / 32; ++kc) {
      asm volatile("s_waitcnt vmcnt(0)" ::: "memory");
      __syncthreads();
      if (kc + 1 < DM / 32) stageW(kc + 1, (kc + 1) & 1);
      const char* wb = sm + VBUF + (kc & 1) * 16384 + wrd;
      bf16x8 af[2];
#pragma unroll
      for (int d = 0; d < 2; ++d) af[d] = cat(tr_read(wb + d * 256), tr_read(wb + d * 256 + 4096));
#pragma unroll
      for (int rt = 0; rt < 6; ++rt) {
        bf16x8 bfr = *reinterpret_cast<const bf16x8*>(xg + (size_t)(rt * 16 + i) * DM + kc * 32 + 8 * g);
#pragma unroll
        for (int d = 0; d < 2; ++d) acc[d][rt] = mfma16(af[d], bfr, acc[d][rt]);
      }
    }
#pragma unroll
    for (int d = 0; d < 2; ++d)
#pragma unroll
      for (int rt = 0; rt < 6; ++rt) {
        u32x2 v = {pk2(acc[d][rt][0], acc[d][rt][1]), pk2(acc[d][rt][2], acc[d][rt][3])};
        char* qb = (rt < 4) ? (sm + KBUF + rt * 8192) : (sm + SCR + (rt - 4) * 8192);
        *reinterpret_cast<u32x2*>(qb + wu * 1024 + i * 64 + (((d * 2 + (g >> 1)) ^ fi) << 4) + 8 * (g & 1)) = v;
      }
  }
  __syncthreads();
  const int a = wu & 3, kh = wu >> 2, st = 4 + (a >> 1), qt = a & 1;
  const int frd = i * 64 + ((g ^ fi) << 4);
  bf16x8 qo[8];
#pragma unroll
  for (int u = 0; u < 8; ++u) qo[u] = *reinterpret_cast<const bf16x8*>(sm + KBUF + a * 8192 + u * 1024 + frd);
  __syncthreads();

  const __amdgpu_buffer_rsrc_t krs = mk_rsrc(kg, SK * DH * 2), vrs = mk_rsrc(vg, SK * DH * 2);
  const int kvo = ((tid >> 2) & 15) * 512 + wu * 64 + (((tid & 3) ^ ((0x1320 >> (((tid >> 4) & 3) * 4)) & 3)) << 4);
  const int vvo = ((tid >> 8) * 8 + ((tid >> 1) & 7)) * 512 + (((tid >> 4) & 15) * 2 + (tid & 1)) * 16;
  auto stageK = [&](int t) {
#pragma unroll
    for (int it = 0; it < 8; ++it) bdma16(krs, kvo, t * (MT * 512) + it * 8192, sm + KBUF + it * 8192 + wu * 1024);
  };
  auto stageV = [&](int t) {
#pragma unroll
    for (int it = 0; it < 8; ++it) bdma16(vrs, vvo, t * (MT * 512) + it * 8192, sm + VBUF + it * 8192 + wu * 1024);
  };

  f32x4 oo[16], os[16];
#pragma unroll
  for (int d = 0; d < 16; ++d) { oo[d] = f32x4{0.f, 0.f, 0.f, 0.f}; os[d] = f32x4{0.f, 0.f, 0.f, 0.f}; }
  float mo = -INFINITY, ms = -INFINITY, lo_ = 0.f, ls_ = 0.f;

  stageK(0);
  stageV(0);
  asm volatile("s_waitcnt vmcnt(8)" ::: "memory");
  __syncthreads();

  const char* kA = sm + KBUF + (kh * 4 + qt * 2) * 8192 + frd;
  const char* kB = sm + KBUF + (kh * 4 + (qt ^ 1) * 2) * 8192 + frd;
  const char* qsb = sm + SCR + (st - 4) * 8192 + frd;
  const int vrd = (l >> 5) * 4096 + ((l >> 2) & 7) * 32 + (l & 3) * 8;
  const char* vA = sm + VBUF + (kh * 8 + qt * 4) * 4096 + vrd;
  const char* vB = sm + VBUF + (kh * 8 + (qt ^ 1) * 4) * 4096 + vrd;

  for (int t = 0; t < NT; ++t) {
    f32x4 so[4], ss[2];
#pragma unroll
    for (int kt = 0; kt < 4; ++kt) so[kt] = f32x4{0.f, 0.f, 0.f, 0.f};
    ss[0] = f32x4{0.f, 0.f, 0.f, 0.f}; ss[1] = f32x4{0.f, 0.f, 0.f, 0.f};
#pragma unroll
    for (int u = 0; u < 8; ++u) {
      const bf16x8 qsu = *reinterpret_cast<const bf16x8*>(qsb + u * 1024);
#pragma unroll
      for (int kt = 0; kt < 4; ++kt) {
        bf16x8 kf = *reinterpret_cast<const bf16x8*>((kt < 2 ? kA : kB) + (kt & 1) * 8192 + u * 1024);
        so[kt] = mfma16(kf, qo[u], so[kt]);
        if (kt < 2) ss[kt] = mfma16(kf, qsu, ss[kt]);
      }
    }
    asm volatile("s_waitcnt vmcnt(0)" ::: "memory");
    __syncthreads();
    if (t + 1 < NT) stageK(t + 1);

    bf16x8 po[2], ps;
    {
      float mx = so[0][0];
#pragma unroll
      for (int kt = 0; kt < 4; ++kt)
#pragma unroll
        for (int r = 0; r < 4; ++r) mx = fmaxf(mx, so[kt][r]);
      mx = fmaxf(mx, __shfl_xor(mx, 16));
      mx = fmaxf(mx, __shfl_xor(mx, 32));
      const float mn = fmaxf(mo, mx);
      const float al = __builtin_amdgcn_exp2f((mo - mn) * SCL);
      mo = mn;
      const float mc = -mn * SCL;
      float sum = 0.f;
      unsigned pkd[8];
#pragma unroll
      for (int kt = 0; kt < 4; ++kt) {
        float p0 = __builtin_amdgcn_exp2f(fmaf(so[kt][0], SCL, mc)), p1 = __builtin_amdgcn_exp2f(fmaf(so[kt][1], SCL, mc));
        float p2 = __builtin_amdgcn_exp2f(fmaf(so[kt][2], SCL, mc)), p3 = __builtin_amdgcn_exp2f(fmaf(so[kt][3], SCL, mc));
        sum += (p0 + p1) + (p2 + p3);
        pkd[2 * kt] = pk2(p0, p1); pkd[2 * kt + 1] = pk2(p2, p3);
      }
      lo_ = lo_ * al + sum;
      po[0] = *reinterpret_cast<bf16x8*>(&pkd[0]);
      po[1] = *reinterpret_cast<bf16x8*>(&pkd[4]);
      if (__any(al < 1.f)) {
#pragma unroll
        for (int d = 0; d < 16; ++d) oo[d] *= al;
      }
    }
    {
      float mx = ss[0][0];
#pragma unroll
      for (int kt = 0; kt < 2; ++kt)
#pragma unroll
        for (int r = 0; r < 4; ++r) mx = fmaxf(mx, ss[kt][r]);
      mx = fmaxf(mx, __shfl_xor(mx, 16));
      mx = fmaxf(mx, __shfl_xor(mx, 32));
      const float mn = fmaxf(ms, mx);
      const float al = __builtin_amdgcn_exp2f((ms - mn) * SCL);
      ms = mn;
      const float mc = -mn * SCL;
      float sum = 0.f;
      unsigned pkd[4];
#pragma unroll
      for (int kt = 0; kt < 2; ++kt) {
        float p0 = __builtin_amdgcn_exp2f(fmaf(ss[kt][0], SCL, mc)), p1 = __builtin_amdgcn_exp2f(fmaf(ss[kt][1], SCL, mc));
        float p2 = __builtin_amdgcn_exp2f(fmaf(ss[kt][2], SCL, mc)), p3 = __builtin_amdgcn_exp2f(fmaf(ss[kt][3], SCL, mc));
        sum += (p0 + p1) + (p2 + p3);
        pkd[2 * kt] = pk2(p0, p1); pkd[2 * kt + 1] = pk2(p2, p3);
      }
      ls_ = ls_ * al + sum;
      ps = *reinterpret_cast<bf16x8*>(&pkd[0]);
      if (__any(al < 1.f)) {
#pragma unroll
        for (int d = 0; d < 16; ++d) os[d] *= al;
      }
    }

#pragma unroll
    for (int dt = 0; dt < 16; ++dt) {
      {
        bf16x8 vf = cat(tr_read(vA + dt * 256), tr_read(vA + dt * 256 + 8192));
        oo[dt] = mfma16(vf, po[0], oo[dt]);
        os[dt] = mfma16(vf, ps, os[dt]);
      }
      {
        bf16x8 vf = cat(tr_read(vB + dt * 256), tr_read(vB + dt * 256 + 8192));
        oo[dt] = mfma16(vf, po[1], oo[dt]);
      }
    }
    asm volatile("s_waitcnt vmcnt(0)" ::: "memory");
    __syncthreads();
    if (t + 1 < NT) stageV(t + 1);
  }

  lo_ += __shfl_xor(lo_, 16); lo_ += __shfl_xor(lo_, 32);
  ls_ += __shfl_xor(ls_, 16); ls_ += __shfl_xor(ls_, 32);
  float* stat = reinterpret_cast<float*>(sm + SCR + 16384);
  if (g == 0) {
    stat[(w * 4 + 0) * 16 + i] = mo; stat[(w * 4 + 1) * 16 + i] = lo_;
    stat[(w * 4 + 2) * 16 + i] = ms; stat[(w * 4 + 3) * 16 + i] = ls_;
  }
  __syncthreads();
  float fo, fs, rlo, rls;
  {
    const int wp = w ^ 4;
    const float m2 = stat[(wp * 4 + 0) * 16 + i], l2 = stat[(wp * 4 + 1) * 16 + i];
    const float mg = fmaxf(mo, m2);
    fo = __builtin_amdgcn_exp2f((mo - mg) * SCL);
    const float lt = lo_ * fo + l2 * __builtin_amdgcn_exp2f((m2 - mg) * SCL);
    rlo = 1.f / lt;
    float mg2 = -INFINITY;
#pragma unroll
    for (int q = 0; q < 4; ++q) { const int wq_ = (a & 2) + (q & 1) + 4 * (q >> 1); mg2 = fmaxf(mg2, stat[(wq_ * 4 + 2) * 16 + i]); }
    float lt2 = 0.f;
#pragma unroll
    for (int q = 0; q < 4; ++q) { const int wq_ = (a & 2) + (q & 1) + 4 * (q >> 1);
      lt2 += stat[(wq_ * 4 + 3) * 16 + i] * __builtin_amdgcn_exp2f((stat[(wq_ * 4 + 2) * 16 + i] - mg2) * SCL); }
    fs = __builtin_amdgcn_exp2f((ms - mg2) * SCL);
    rls = 1.f / lt2;
  }
  fo *= rlo; fs *= rls;
#pragma unroll
  for (int d = 0; d < 16; ++d) { oo[d] *= fo; os[d] *= fs; }
  f32x4* red = reinterpret_cast<f32x4*>(sm);
  if (kh == 1) {
#pragma unroll
    for (int d = 0; d < 16; ++d) { red[((a) * 16 + d) * 64 + l] = oo[d]; red[((4 + a) * 16 + d) * 64 + l] = os[d]; }
  }
  __syncthreads();
  if (kh == 0) {
#pragma unroll
    for (int d = 0; d < 16; ++d) { oo[d] += red[((a) * 16 + d) * 64 + l]; os[d] += red[((4 + a) * 16 + d) * 64 + l]; }
  }
  __syncthreads();
  if (kh == 0 && qt == 1) {
#pragma unroll
    for (int d = 0; d < 16; ++d) red[((a >> 1) * 16 + d) * 64 + l] = os[d];
  }
  __syncthreads();
  if (kh == 0) {
    bf16* ob = Ows + ((size_t)(inst * NB + b) * SQ) * (HQ * DH) + h * DH + 4 * g;
#pragma unroll
    for (int d = 0; d < 16; ++d) {
      u32x2 v = {pk2(oo[d][0], oo[d][1]), pk2(oo[d][2], oo[d][3])};
      *reinterpret_cast<u32x2*>(ob + (size_t)(a * 16 + i) * (HQ * DH) + d * 16) = v;
    }
    if (qt == 0) {
#pragma unroll
      for (int d = 0; d < 16; ++d) {
        f32x4 o2 = os[d] + red[((a >> 1) * 16 + d) * 64 + l];
        u32x2 v = {pk2(o2[0], o2[1]), pk2(o2[2], o2[3])};
        *reinterpret_cast<u32x2*>(ob + (size_t)(st * 16 + i) * (HQ * DH) + d * 16) = v;
      }
    }
  }
}

constexpr int OBM = 48, OBN = 64, OBK = 64;
__global__ __launch_bounds__(256) void oproj_kernel(Ptrs2 P, const bf16* __restrict__ Ows, bf16* __restrict__ out) {
  __shared__ __attribute__((aligned(16))) char sm[2 * (OBM * OBK * 2 + OBK * OBN * 2)];
  const int tid = threadIdx.x, w = tid >> 6, l = tid & 63, g = l >> 4, i = l & 15;
  const int bid = blockIdx.x;
  const int inst = bid / 112, rem = bid % 112, mt = rem / 7, nt = rem % 7;
  const bf16* ag = Ows + ((size_t)inst * NB * SQ + mt * OBM) * (HQ * DH);
  const bf16* bg = P.wo[inst] + nt * OBN;
  constexpr int ABYTES = OBM * OBK * 2, BBYTES = OBK * OBN * 2, BUF = ABYTES + BBYTES;
  auto stage = [&](int kc, int buf) {
    char* base = sm + buf * BUF;
    {
      const int p = tid;
      const int row = p >> 3, cp = p & 7, c = cp ^ (row & 7);
      dma16(ag + (size_t)row * (HQ * DH) + kc * OBK + c * 8, base + p * 16);
      if (w < 2) {
        const int p2 = 256 + tid, row2 = p2 >> 3, c2 = (p2 & 7) ^ (row2 & 7);
        dma16(ag + (size_t)row2 * (HQ * DH) + kc * OBK + c2 * 8, base + p2 * 16);
      }
    }
#pragma unroll
    for (int it = 0; it < 2; ++it) {
      const int p = it * 256 + tid, row = p >> 3, cp = p & 7;
      const int f = ((row >> 1) & 1) | ((row >> 2) & 2);
      const int c = cp ^ (f << 1);
      dma16(bg + (size_t)(kc * OBK + row) * DM + c * 8, base + ABYTES + p * 16);
    }
  };
  f32x4 acc[3];
#pragma unroll
  for (int m = 0; m < 3; ++m) acc[m] = f32x4{0.f, 0.f, 0.f, 0.f};
  stage(0, 0);
  constexpr int NKC = HQ * DH / OBK;
  for (int kc = 0; kc < NKC; ++kc) {
    asm volatile("s_waitcnt vmcnt(0)" ::: "memory");
    __syncthreads();
    if (kc + 1 < NKC) stage(kc + 1, (kc + 1) & 1);
    const char* ab = sm + (kc & 1) * BUF;
    const char* bb = ab + ABYTES;
#pragma unroll
    for (int u = 0; u < 2; ++u) {
      const int r0 = 32 * u + 8 * g + (i >> 2), r1 = r0 + 4;
      const int ch = 2 * w + ((l & 3) >> 1);
      const int f0 = ((r0 >> 1) & 1) | ((r0 >> 2) & 2), f1 = ((r1 >> 1) & 1) | ((r1 >> 2) & 2);
      s16x4 lo = tr_read(bb + r0 * 128 + ((ch ^ (f0 << 1)) << 4) + 8 * (l & 1));
      s16x4 hi = tr_read(bb + r1 * 128 + ((ch ^ (f1 << 1)) << 4) + 8 * (l & 1));
      bf16x8 bfr = cat(lo, hi);
#pragma unroll
      for (int m = 0; m < 3; ++m) {
        const int row = m * 16 + i;
        bf16x8 af = *reinterpret_cast<const bf16x8*>(ab + row * 128 + (((4 * u + g) ^ (row & 7)) << 4));
        acc[m] = mfma16(af, bfr, acc[m]);
      }
    }
  }
#pragma unroll
  for (int m = 0; m < 3; ++m)
#pragma unroll
    for (int r = 0; r < 4; ++r) {
      const int row = mt * OBM + m * 16 + 4 * g + r;
      out[(size_t)row * (2 * DM) + inst * DM + nt * OBN + w * 16 + i] = __float2bfloat16(acc[m][r]);
    }
}

extern "C" void kernel_launch(void* const* d_in, const int* in_sizes, int n_in,
                              void* d_out, int out_size, void* d_ws, size_t ws_size,
                              hipStream_t stream) {
  Ptrs p;
  p.x[0] = (const bf16*)d_in[0]; p.k[0] = (const bf16*)d_in[1]; p.v[0] = (const bf16*)d_in[2]; p.wq[0] = (const bf16*)d_in[3];
  p.x[1] = (const bf16*)d_in[5]; p.k[1] = (const bf16*)d_in[6]; p.v[1] = (const bf16*)d_in[7]; p.wq[1] = (const bf16*)d_in[8];
  Ptrs2 p2;
  p2.wo[0] = (const bf16*)d_in[4]; p2.wo[1] = (const bf16*)d_in[9];
  bf16* ows = (bf16*)d_ws;
  (void)hipFuncSetAttribute((const void*)attn_kernel, hipFuncAttributeMaxDynamicSharedMemorySize, LDS_BYTES);
  attn_kernel<<<256, 512, LDS_BYTES, stream>>>(p, ows);
  oproj_kernel<<<224, 256, 0, stream>>>(p2, ows, (bf16*)d_out);
}
```

```cpp
#include <hip/hip_runtime.h>
#include <hip/hip_bf16.h>

typedef __hip_bfloat16 bf16;
using bf16x8 = __attribute__((ext_vector_type(8))) short;
using s16x4  = __attribute__((ext_vector_type(4))) short;
using f32x4  = __attribute__((ext_vector_type(4))) float;
using u32x2  = __attribute__((ext_vector_type(2))) unsigned;
typedef __attribute__((address_space(3))) s16x4* lds_s16x4_p;

constexpr int NB = 8, SQ = 96, DM = 448, HQ = 16, HKV = 8, SK = 1024, DH = 256;
constexpr int MT = 128;
constexpr int NT = SK / MT;
constexpr int KBUF = 0, VBUF = 65536, SCR = 131072, LDS_BYTES = 163840;
constexpr float SCL = 0.0625f * 1.4426950408889634f;

struct Ptrs { const bf16* x[2]; const bf16* k[2]; const bf16* v[2]; const bf16* wq[2]; };
struct Ptrs2 { const bf16* wo[2]; };

__device__ __forceinline__ void dma16(const void* src, void* lds) {
  __builtin_amdgcn_global_load_lds((const unsigned*)src, (unsigned*)lds, 16, 0, 0);
}
typedef __attribute__((address_space(3))) void* lds_void_p;
__device__ __forceinline__ __amdgpu_buffer_rsrc_t mk_rsrc(const void* p, unsigned bytes) {
  return __builtin_amdgcn_make_buffer_rsrc(const_cast<void*>(p), 0, bytes, 0x00020000);
}
__device__ __forceinline__ void bdma16(__amdgpu_buffer_rsrc_t r, int voff, int soff, void* lds) {
  __builtin_amdgcn_raw_ptr_buffer_load_lds(r, (lds_void_p)lds, 16, voff, soff, 0, 0);
}
__device__ __forceinline__ s16x4 tr_read(const char* p) {
  return __builtin_amdgcn_ds_read_tr16_b64_v4i16((lds_s16x4_p)p);
}
__device__ __forceinline__ unsigned pk2(float lo, float hi) {
  union { __hip_bfloat162 h; unsigned u; } c;
  c.h = __float22bfloat162_rn(float2{lo, hi});
  return c.u;
}
__device__ __forceinline__ f32x4 mfma16(bf16x8 a, bf16x8 b, f32x4 c) {
  return __builtin_amdgcn_mfma_f32_16x16x32_bf16(a, b, c, 0, 0, 0);
}
__device__ __forceinline__ bf16x8 cat(s16x4 l, s16x4 h) {
  return (bf16x8){l[0], l[1], l[2], l[3], h[0], h[1], h[2], h[3]};
}

__global__ __launch_bounds__(512, 2) void attn_kernel(Ptrs P, bf16* __restrict__ Ows) {
  extern __shared__ __attribute__((aligned(16))) char sm[];
  const int tid = threadIdx.x, w = tid >> 6, l = tid & 63, g = l >> 4, i = l & 15;
  const int bid = blockIdx.x;
  const int grp = ((bid >> 4) << 3) | (bid & 7), pr = (bid >> 3) & 1;
  const int inst = grp >> 6, b = (grp >> 3) & 7, kvh = grp & 7, h = kvh * 2 + pr;
  const bf16* xg = P.x[inst] + (size_t)b * SQ * DM;
  const bf16* wg = P.wq[inst] + h * DH;
  const bf16* kg = P.k[inst] + (size_t)(b * HKV + kvh) * SK * DH;
  const bf16* vg = P.v[inst] + (size_t)(b * HKV + kvh) * SK * DH;

  const int wu = __builtin_amdgcn_readfirstlane(w);
  const int fi = (0x1320 >> ((i >> 2) * 4)) & 3;

  {
    f32x4 acc[2][6];
#pragma unroll
    for (int d = 0; d < 2; ++d)
#pragma unroll
      for (int r = 0; r < 6; ++r) acc[d][r] = f32x4{0.f, 0.f, 0.f, 0.f};
    const __amdgpu_buffer_rsrc_t wrs = mk_rsrc(wg, (DM * HQ * DH - h * DH) * 2), xrs = mk_rsrc(xg, SQ * DM * 2);
    int wvo;
    {
      const int kp = (tid >> 8) * 8 + ((tid >> 1) & 7);
      const int kr = (kp & 3) | ((kp & 4) << 1) | ((kp & 8) >> 1);
      wvo = kr * (HQ * DH * 2) + (((tid >> 4) & 15) * 2 + (tid & 1)) * 16;
    }
    const int xvo = (tid >> 3) * (DM * 2) + (((tid & 7) ^ ((tid >> 3) & 7)) << 4);
    constexpr int QST = 49152, NQC = DM / 64;
    auto stageQ = [&](int kc, int buf) {
      char* base = sm + buf * QST + wu * 1024;
#pragma unroll
      for (int it = 0; it < 4; ++it) bdma16(wrs, wvo, (kc * 64 + it * 16) * (HQ * DH * 2), base + it * 8192);
#pragma unroll
      for (int it = 0; it < 2; ++it) bdma16(xrs, xvo, it * 64 * (DM * 2) + kc * 128, base + 32768 + it * 8192);
    };
    stageQ(0, 0);
    stageQ(1, 1);
    const int wrd = (g >> 1) * 8192 + ((g & 1) * 4 + (i >> 2)) * 32 + (l & 3) * 8 + wu * 512;
    const int xrd0 = 32768 + i * 128 + (((g) ^ (i & 7)) << 4), xrd1 = 32768 + i * 128 + (((4 + g) ^ (i & 7)) << 4);
#pragma unroll
    for (int kc = 0; kc < NQC; ++kc) {
      if (kc + 1 < NQC) asm volatile("s_waitcnt vmcnt(6)" ::: "memory"); else asm volatile("s_waitcnt vmcnt(0)" ::: "memory");
      __builtin_amdgcn_s_barrier();
      if (kc + 2 < NQC) stageQ(kc + 2, (kc + 2) % 3);
      const char* sb = sm + (kc % 3) * QST;
#pragma unroll
      for (int u = 0; u < 2; ++u) {
        bf16x8 af[2];
#pragma unroll
        for (int d = 0; d < 2; ++d) af[d] = cat(tr_read(sb + wrd + u * 16384 + d * 256), tr_read(sb + wrd + u * 16384 + d * 256 + 4096));
#pragma unroll
        for (int rt = 0; rt < 6; ++rt) {
          bf16x8 bfr = *reinterpret_cast<const bf16x8*>(sb + (u ? xrd1 : xrd0) + rt * 2048);
#pragma unroll
          for (int d = 0; d < 2; ++d) acc[d][rt] = mfma16(af[d], bfr, acc[d][rt]);
        }
      }
      asm volatile("s_waitcnt lgkmcnt(0)" ::: "memory");
    }
    __builtin_amdgcn_s_barrier();
#pragma unroll
    for (int d = 0; d < 2; ++d)
#pragma unroll
      for (int rt = 0; rt < 6; ++rt) {
        u32x2 v = {pk2(acc[d][rt][0], acc[d][rt][1]), pk2(acc[d][rt][2], acc[d][rt][3])};
        char* qb = (rt < 4) ? (sm + KBUF + rt * 8192) : (sm + SCR + (rt - 4) * 8192);
        *reinterpret_cast<u32x2*>(qb + wu * 1024 + i * 64 + (((d * 2 + (g >> 1)) ^ fi) << 4) + 8 * (g & 1)) = v;
      }
  }
  __syncthreads();
  const int a = wu & 3, kh = wu >> 2, st = 4 + (a >> 1), qt = a & 1;
  const int frd = i * 64 + ((g ^ fi) << 4);
  bf16x8 qo[8];
#pragma unroll
  for (int u = 0; u < 8; ++u) qo[u] = *reinterpret_cast<const bf16x8*>(sm + KBUF + a * 8192 + u * 1024 + frd);
  __syncthreads();

  const __amdgpu_buffer_rsrc_t krs = mk_rsrc(kg, SK * DH * 2), vrs = mk_rsrc(vg, SK * DH * 2);
  const int kvo = ((tid >> 2) & 15) * 512 + wu * 64 + (((tid & 3) ^ ((0x1320 >> (((tid >> 4) & 3) * 4)) & 3)) << 4);
  const int vvo = ((tid >> 8) * 8 + ((tid >> 1) & 7)) * 512 + (((tid >> 4) & 15) * 2 + (tid & 1)) * 16;
  auto stageK = [&](int t) {
#pragma unroll
    for (int it = 0; it < 8; ++it) bdma16(krs, kvo, t * (MT * 512) + it * 8192, sm + KBUF + it * 8192 + wu * 1024);
  };
  auto stageV = [&](int t) {
#pragma unroll
    for (int it = 0; it < 8; ++it) bdma16(vrs, vvo, t * (MT * 512) + it * 8192, sm + VBUF + it * 8192 + wu * 1024);
  };

  f32x4 oo[16], os[16];
#pragma unroll
  for (int d = 0; d < 16; ++d) { oo[d] = f32x4{0.f, 0.f, 0.f, 0.f}; os[d] = f32x4{0.f, 0.f, 0.f, 0.f}; }
  float mo = -INFINITY, ms = -INFINITY, lo_ = 0.f, ls_ = 0.f;

  stageK(0);
  stageV(0);
  asm volatile("s_waitcnt vmcnt(8)" ::: "memory");
  __syncthreads();

  const char* kA = sm + KBUF + (kh * 4 + qt * 2) * 8192 + frd;
  const char* kB = sm + KBUF + (kh * 4 + (qt ^ 1) * 2) * 8192 + frd;
  const char* qsb = sm + SCR + (st - 4) * 8192 + frd;
  const int vrd = (l >> 5) * 4096 + ((l >> 2) & 7) * 32 + (l & 3) * 8;
  const char* vA = sm + VBUF + (kh * 8 + qt * 4) * 4096 + vrd;
  const char* vB = sm + VBUF + (kh * 8 + (qt ^ 1) * 4) * 4096 + vrd;

  for (int t = 0; t < NT; ++t) {
    f32x4 so[4], ss[2];
#pragma unroll
    for (int kt = 0; kt < 4; ++kt) so[kt] = f32x4{0.f, 0.f, 0.f, 0.f};
    ss[0] = f32x4{0.f, 0.f, 0.f, 0.f}; ss[1] = f32x4{0.f, 0.f, 0.f, 0.f};
#pragma unroll
    for (int u = 0; u < 8; ++u) {
      const bf16x8 qsu = *reinterpret_cast<const bf16x8*>(qsb + u * 1024);
#pragma unroll
      for (int kt = 0; kt < 4; ++kt) {
        bf16x8 kf = *reinterpret_cast<const bf16x8*>((kt < 2 ? kA : kB) + (kt & 1) * 8192 + u * 1024);
        so[kt] = mfma16(kf, qo[u], so[kt]);
        if (kt < 2) ss[kt] = mfma16(kf, qsu, ss[kt]);
      }
    }
    asm volatile("s_waitcnt vmcnt(0)" ::: "memory");
    __syncthreads();
    if (t + 1 < NT) stageK(t + 1);

    bf16x8 po[2], ps;
    {
      float mx = so[0][0];
#pragma unroll
      for (int kt = 0; kt < 4; ++kt)
#pragma unroll
        for (int r = 0; r < 4; ++r) mx = fmaxf(mx, so[kt][r]);
      mx = fmaxf(mx, __shfl_xor(mx, 16));
      mx = fmaxf(mx, __shfl_xor(mx, 32));
      const float mn = fmaxf(mo, mx);
      const float al = __builtin_amdgcn_exp2f((mo - mn) * SCL);
      mo = mn;
      const float mc = -mn * SCL;
      float sum = 0.f;
      unsigned pkd[8];
#pragma unroll
      for (int kt = 0; kt < 4; ++kt) {
        float p0 = __builtin_amdgcn_exp2f(fmaf(so[kt][0], SCL, mc)), p1 = __builtin_amdgcn_exp2f(fmaf(so[kt][1], SCL, mc));
        float p2 = __builtin_amdgcn_exp2f(fmaf(so[kt][2], SCL, mc)), p3 = __builtin_amdgcn_exp2f(fmaf(so[kt][3], SCL, mc));
        sum += (p0 + p1) + (p2 + p3);
        pkd[2 * kt] = pk2(p0, p1); pkd[2 * kt + 1] = pk2(p2, p3);
      }
      lo_ = lo_ * al + sum;
      po[0] = *reinterpret_cast<bf16x8*>(&pkd[0]);
      po[1] = *reinterpret_cast<bf16x8*>(&pkd[4]);
      if (__any(al < 1.f)) {
#pragma unroll
        for (int d = 0; d < 16; ++d) oo[d] *= al;
      }
    }
    {
      float mx = ss[0][0];
#pragma unroll
      for (int kt = 0; kt < 2; ++kt)
#pragma unroll
        for (int r = 0; r < 4; ++r) mx = fmaxf(mx, ss[kt][r]);
      mx = fmaxf(mx, __shfl_xor(mx, 16));
      mx = fmaxf(mx, __shfl_xor(mx, 32));
      const float mn = fmaxf(ms, mx);
      const float al = __builtin_amdgcn_exp2f((ms - mn) * SCL);
      ms = mn;
      const float mc = -mn * SCL;
      float sum = 0.f;
      unsigned pkd[4];
#pragma unroll
      for (int kt = 0; kt < 2; ++kt) {
        float p0 = __builtin_amdgcn_exp2f(fmaf(ss[kt][0], SCL, mc)), p1 = __builtin_amdgcn_exp2f(fmaf(ss[kt][1], SCL, mc));
        float p2 = __builtin_amdgcn_exp2f(fmaf(ss[kt][2], SCL, mc)), p3 = __builtin_amdgcn_exp2f(fmaf(ss[kt][3], SCL, mc));
        sum += (p0 + p1) + (p2 + p3);
        pkd[2 * kt] = pk2(p0, p1); pkd[2 * kt + 1] = pk2(p2, p3);
      }
      ls_ = ls_ * al + sum;
      ps = *reinterpret_cast<bf16x8*>(&pkd[0]);
      if (__any(al < 1.f)) {
#pragma unroll
        for (int d = 0; d < 16; ++d) os[d] *= al;
      }
    }

#pragma unroll
    for (int dt = 0; dt < 16; ++dt) {
      {
        bf16x8 vf = cat(tr_read(vA + dt * 256), tr_read(vA + dt * 256 + 8192));
        oo[dt] = mfma16(vf, po[0], oo[dt]);
        os[dt] = mfma16(vf, ps, os[dt]);
      }
      {
        bf16x8 vf = cat(tr_read(vB + dt * 256), tr_read(vB + dt * 256 + 8192));
        oo[dt] = mfma16(vf, po[1], oo[dt]);
      }
    }
    asm volatile("s_waitcnt vmcnt(0)" ::: "memory");
    __syncthreads();
    if (t + 1 < NT) stageV(t + 1);
  }

  lo_ += __shfl_xor(lo_, 16); lo_ += __shfl_xor(lo_, 32);
  ls_ += __shfl_xor(ls_, 16); ls_ += __shfl_xor(ls_, 32);
  float* stat = reinterpret_cast<float*>(sm + SCR + 16384);
  if (g == 0) {
    stat[(w * 4 + 0) * 16 + i] = mo; stat[(w * 4 + 1) * 16 + i] = lo_;
    stat[(w * 4 + 2) * 16 + i] = ms; stat[(w * 4 + 3) * 16 + i] = ls_;
  }
  __syncthreads();
  float fo, fs, rlo, rls;
  {
    const int wp = w ^ 4;
    const float m2 = stat[(wp * 4 + 0) * 16 + i], l2 = stat[(wp * 4 + 1) * 16 + i];
    const float mg = fmaxf(mo, m2);
    fo = __builtin_amdgcn_exp2f((mo - mg) * SCL);
    const float lt = lo_ * fo + l2 * __builtin_amdgcn_exp2f((m2 - mg) * SCL);
    rlo = 1.f / lt;
    float mg2 = -INFINITY;
#pragma unroll
    for (int q = 0; q < 4; ++q) { const int wq_ = (a & 2) + (q & 1) + 4 * (q >> 1); mg2 = fmaxf(mg2, stat[(wq_ * 4 + 2) * 16 + i]); }
    float lt2 = 0.f;
#pragma unroll
    for (int q = 0; q < 4; ++q) { const int wq_ = (a & 2) + (q & 1) + 4 * (q >> 1);
      lt2 += stat[(wq_ * 4 + 3) * 16 + i] * __builtin_amdgcn_exp2f((stat[(wq_ * 4 + 2) * 16 + i] - mg2) * SCL); }
    fs = __builtin_amdgcn_exp2f((ms - mg2) * SCL);
    rls = 1.f / lt2;
  }
  fo *= rlo; fs *= rls;
#pragma unroll
  for (int d = 0; d < 16; ++d) { oo[d] *= fo; os[d] *= fs; }
  f32x4* red = reinterpret_cast<f32x4*>(sm);
  if (kh == 1) {
#pragma unroll
    for (int d = 0; d < 16; ++d) { red[((a) * 16 + d) * 64 + l] = oo[d]; red[((4 + a) * 16 + d) * 64 + l] = os[d]; }
  }
  __syncthreads();
  if (kh == 0) {
#pragma unroll
    for (int d = 0; d < 16; ++d) { oo[d] += red[((a) * 16 + d) * 64 + l]; os[d] += red[((4 + a) * 16 + d) * 64 + l]; }
  }
  __syncthreads();
  if (kh == 0 && qt == 1) {
#pragma unroll
    for (int d = 0; d < 16; ++d) red[((a >> 1) * 16 + d) * 64 + l] = os[d];
  }
  __syncthreads();
  if (kh == 0) {
    bf16* ob = Ows + ((size_t)(inst * NB + b) * SQ) * (HQ * DH) + h * DH + 4 * g;
#pragma unroll
    for (int d = 0; d < 16; ++d) {
      u32x2 v = {pk2(oo[d][0], oo[d][1]), pk2(oo[d][2], oo[d][3])};
      *reinterpret_cast<u32x2*>(ob + (size_t)(a * 16 + i) * (HQ * DH) + d * 16) = v;
    }
    if (qt == 0) {
#pragma unroll
      for (int d = 0; d < 16; ++d) {
        f32x4 o2 = os[d] + red[((a >> 1) * 16 + d) * 64 + l];
        u32x2 v = {pk2(o2[0], o2[1]), pk2(o2[2], o2[3])};
        *reinterpret_cast<u32x2*>(ob + (size_t)(st * 16 + i) * (HQ * DH) + d * 16) = v;
      }
    }
  }
}

constexpr int OBM = 48, OBN = 64, OBK = 128, ONS = 4;
constexpr int OAB = OBM * OBK * 2, OBB = OBK * OBN * 2, OBUF = OAB + OBB;
__global__ __launch_bounds__(256) void oproj_kernel(Ptrs2 P, const bf16* __restrict__ Ows, bf16* __restrict__ out) {
  extern __shared__ __attribute__((aligned(16))) char sm[];
  const int tid = threadIdx.x, w = tid >> 6, l = tid & 63, g = l >> 4, i = l & 15;
  const int wu = __builtin_amdgcn_readfirstlane(w);
  const int bid = blockIdx.x;
  const int xc = bid & 7, jj = bid >> 3;
  const int inst = xc >> 2, mt = (xc & 3) * 4 + jj / 7, nt = jj % 7;
  const bf16* ag = Ows + ((size_t)inst * NB * SQ + mt * OBM) * (HQ * DH);
  const bf16* bg = P.wo[inst] + nt * OBN;
  const __amdgpu_buffer_rsrc_t ars = mk_rsrc(ag, OBM * HQ * DH * 2), brs = mk_rsrc(bg, (HQ * DH * DM - nt * OBN) * 2);
  const int avo = (tid >> 4) * (HQ * DH * 2) + (((tid & 15) ^ ((tid >> 4) & 15)) << 4);
  int bvo;
  { const int row = tid >> 3, f = ((row >> 1) & 1) | ((row >> 2) & 2); bvo = row * (DM * 2) + (((tid & 7) ^ (f << 1)) << 4); }
  auto stage = [&](int kc, int buf) {
    char* base = sm + buf * OBUF + wu * 1024;
#pragma unroll
    for (int it = 0; it < 3; ++it) bdma16(ars, avo, it * 16 * (HQ * DH * 2) + kc * (OBK * 2), base + it * 4096);
#pragma unroll
    for (int it = 0; it < 4; ++it) bdma16(brs, bvo, (kc * OBK + it * 32) * (DM * 2), base + OAB + it * 4096);
  };
  f32x4 acc[3];
#pragma unroll
  for (int m = 0; m < 3; ++m) acc[m] = f32x4{0.f, 0.f, 0.f, 0.f};
  stage(0, 0); stage(1, 1); stage(2, 2);
  constexpr int NKC = HQ * DH / OBK;
  int brd0, brd1;
  {
    const int r0 = 8 * g + (i >> 2), r1 = r0 + 4, ch = 2 * w + ((l & 3) >> 1);
    const int f0 = ((r0 >> 1) & 1) | ((r0 >> 2) & 2), f1 = ((r1 >> 1) & 1) | ((r1 >> 2) & 2);
    brd0 = OAB + r0 * 128 + ((ch ^ (f0 << 1)) << 4) + 8 * (l & 1);
    brd1 = OAB + r1 * 128 + ((ch ^ (f1 << 1)) << 4) + 8 * (l & 1);
  }
  for (int kc = 0; kc < NKC; ++kc) {
    if (kc + 2 < NKC) asm volatile("s_waitcnt vmcnt(14)" ::: "memory");
    else if (kc + 1 < NKC) asm volatile("s_waitcnt vmcnt(7)" ::: "memory");
    else asm volatile("s_waitcnt vmcnt(0)" ::: "memory");
    __builtin_amdgcn_s_barrier();
    if (kc + 3 < NKC) stage(kc + 3, (kc + 3) & 3);
    const char* sb = sm + (kc & 3) * OBUF;
#pragma unroll
    for (int u = 0; u < 4; ++u) {
      bf16x8 bfr = cat(tr_read(sb + brd0 + u * 4096), tr_read(sb + brd1 + u * 4096));
#pragma unroll
      for (int m = 0; m < 3; ++m) {
        bf16x8 af = *reinterpret_cast<const bf16x8*>(sb + (m * 16 + i) * 256 + (((4 * u + g) ^ i) << 4));
        acc[m] = mfma16(af, bfr, acc[m]);
      }
    }
    asm volatile("s_waitcnt lgkmcnt(0)" ::: "memory");
  }
#pragma unroll
  for (int m = 0; m < 3; ++m)
#pragma unroll
    for (int r = 0; r < 4; ++r) {
      const int row = mt * OBM + m * 16 + 4 * g + r;
      out[(size_t)row * (2 * DM) + inst * DM + nt * OBN + w * 16 + i] = __float2bfloat16(acc[m][r]);
    }
}

extern "C" void kernel_launch(void* const* d_in, const int* in_sizes, int n_in,
                              void* d_out, int out_size, void* d_ws, size_t ws_size,
                              hipStream_t stream) {
  Ptrs p;
  p.x[0] = (const bf16*)d_in[0]; p.k[0] = (const bf16*)d_in[1]; p.v[0] = (const bf16*)d_in[2]; p.wq[0] = (const bf16*)d_in[3];
  p.x[1] = (const bf16*)d_in[5]; p.k[1] = (const bf16*)d_in[6]; p.v[1] = (const bf16*)d_in[7]; p.wq[1] = (const bf16*)d_in[8];
  Ptrs2 p2;
  p2.wo[0] = (const bf16*)d_in[4]; p2.wo[1] = (const bf16*)d_in[9];
  bf16* ows = (bf16*)d_ws;
  (void)hipFuncSetAttribute((const void*)attn_kernel, hipFuncAttributeMaxDynamicSharedMemorySize, LDS_BYTES);
  attn_kernel<<<256, 512, LDS_BYTES, stream>>>(p, ows);
  (void)hipFuncSetAttribute((const void*)oproj_kernel, hipFuncAttributeMaxDynamicSharedMemorySize, ONS * OBUF);
  oproj_kernel<<<224, 256, ONS * OBUF, stream>>>(p2, ows, (bf16*)d_out);
}
```
